```python
import torch

def _softmax(x, axis=-1):
    return torch.softmax(x, dim=axis)
def _gelu(x):
    return torch.nn.functional.gelu(x, approximate="tanh")
def _silu(x):
    return torch.nn.functional.silu(x)
def _sigmoid(x):
    return torch.sigmoid(x)
def _layernorm(x, axis=-1, eps=1e-5):
    m = x.mean(dim=axis, keepdim=True)
    v = x.var(dim=axis, keepdim=True, unbiased=False)
    return (x - m) / torch.sqrt(v + eps)
def _scatter_add(tgt, idx, src, axis=0):
    return tgt.clone().index_add_(axis, idx, src)
def _segment_sum(src, seg, n):
    out = torch.zeros((n,) + src.shape[1:], dtype=src.dtype)
    return out.index_add_(0, seg, src)
def _pad(x, pw):
    flat = tuple(v for pair in reversed(pw) for v in pair)
    return torch.nn.functional.pad(x, flat)

kernel_name = 'parallel_sum__mha_block_cached__swiglu_ffn__decode'

def setup_inputs():

    _w = (lambda a: (lambda t: t.to(torch.bfloat16) if t.is_floating_point() else t)(torch.as_tensor(a)))
    def _A_setup():
        B, Sq, D = 4, 1, 640
        H, Sk, Dh = 32, 4096, 128
        return {
            "x": _w((lambda *s: torch.randn(*s, dtype=torch.bfloat16) * 0.05)(B, Sq, D)),
            "k_cache": _w((lambda *s: torch.randn(*s, dtype=torch.bfloat16) * 3.2)(B, H, Sk, Dh)),
            "v_cache": _w((lambda *s: torch.randn(*s, dtype=torch.bfloat16) * 1.6)(B, H, Sk, Dh)),
            "wq": _w((lambda *s: torch.randn(*s, dtype=torch.bfloat16) * 3.2)(D, H * Dh) * (D ** -0.5)),
            "wo": _w((lambda *s: torch.randn(*s, dtype=torch.bfloat16) * 1.6)(H * Dh, D) * ((H * Dh) ** -0.5)),
        }
    _A_in = _A_setup()

    def _B_setup():
        B, S, D, F = 4, 1, 640, 1728
        return {
            "x": _w((lambda *s: torch.randn(*s, dtype=torch.bfloat16) * 0.05)(B, S, D)),
            "w_gate": _w((lambda *s: torch.randn(*s, dtype=torch.bfloat16) * 0.05)(D, F)),
            "w_up": _w((lambda *s: torch.randn(*s, dtype=torch.bfloat16) * 0.05)(D, F)),
            "w_down": _w((lambda *s: torch.randn(*s, dtype=torch.bfloat16) * 0.05)(F, D)),
        }
    _B_in = _B_setup()

    return {
        "A_x": _A_in["x"],
        "A_k_cache": _A_in["k_cache"],
        "A_v_cache": _A_in["v_cache"],
        "A_wq": _A_in["wq"],
        "A_wo": _A_in["wo"],
        "B_x": _B_in["x"],
        "B_w_gate": _B_in["w_gate"],
        "B_w_up": _B_in["w_up"],
        "B_w_down": _B_in["w_down"],
    }

def reference(A_x, A_k_cache, A_v_cache, A_wq, A_wo, B_x, B_w_gate, B_w_up, B_w_down):
    def _A_ref(x, k_cache, v_cache, wq, wo):
        B, Sq, D = x.shape
        H, Dh = 32, 128
        q = (lambda x, *d: x.permute(*d))((x @ wq).reshape(B, Sq, H, Dh), 0, 2, 1, 3)
        scores = torch.einsum("bhqd,bhkd->bhqk", q, k_cache) * (Dh ** -0.5)
        p = _softmax(scores, axis=-1)
        o = torch.einsum("bhqk,bhkd->bhqd", p, v_cache)
        return (lambda x, *d: x.permute(*d))(o, 0, 2, 1, 3).reshape(B, Sq, H * Dh) @ wo

    def _B_ref(x, w_gate, w_up, w_down):
        g = _silu(x @ w_gate)
        u = x @ w_up
        return (g * u) @ w_down

    x = A_x
    xn = _layernorm(x, axis=-1)
    return x + _A_ref(xn, A_k_cache, A_v_cache, A_wq, A_wo) + _B_ref(xn, B_w_gate, B_w_up, B_w_down)
```

```cpp
#include <hip/hip_runtime.h>
#include <hip/hip_bf16.h>
#include <stdint.h>

#define EMUL 1

typedef unsigned u32x4 __attribute__((ext_vector_type(4)));
typedef float f32x4 __attribute__((ext_vector_type(4)));
typedef __bf16 bf16x2 __attribute__((ext_vector_type(2)));

constexpr int B_ = 4, D_ = 640, H_ = 32, SK = 4096, DH = 128, F_ = 1728;
constexpr int NATT = 256, NFFN = 27;

__device__ __forceinline__ float dot2(unsigned a, unsigned b, float acc) {
  return __builtin_amdgcn_fdot2_f32_bf16(__builtin_bit_cast(bf16x2, a), __builtin_bit_cast(bf16x2, b), acc, false);
}
__device__ __forceinline__ float bflo(unsigned u) { return __builtin_bit_cast(float, u << 16); }
__device__ __forceinline__ float bfhi(unsigned u) { return __builtin_bit_cast(float, u & 0xffff0000u); }
__device__ __forceinline__ unsigned cvtpk(float lo, float hi) {
  unsigned r; asm("v_cvt_pk_bf16_f32 %0, %1, %2" : "=v"(r) : "v"(lo), "v"(hi)); return r;
}
__device__ __forceinline__ float rbf(float x) { return bflo(cvtpk(x, x)); }
template <int CTRL> __device__ __forceinline__ float dpp(float x) {
  return __builtin_bit_cast(float, __builtin_amdgcn_mov_dpp(__builtin_bit_cast(int, x), CTRL, 0xf, 0xf, true));
}
constexpr int XOR1 = 0xB1, XOR2 = 0x4E, XOR7 = 0x141, XOR8 = 0x128;
__device__ __forceinline__ float row16_sum(float x) {
  x += dpp<XOR8>(x); x += dpp<XOR7>(x); x += dpp<XOR2>(x); x += dpp<XOR1>(x); return x;
}
__device__ __forceinline__ float row16_max(float x) {
  x = fmaxf(x, dpp<XOR8>(x)); x = fmaxf(x, dpp<XOR7>(x)); x = fmaxf(x, dpp<XOR2>(x)); x = fmaxf(x, dpp<XOR1>(x)); return x;
}
__device__ __forceinline__ float xrow_sum(float x) {
  auto s = __builtin_amdgcn_permlane16_swap(__float_as_uint(x), __float_as_uint(x), false, false);
  x = __uint_as_float(s[0]) + __uint_as_float(s[1]);
  auto t = __builtin_amdgcn_permlane32_swap(__float_as_uint(x), __float_as_uint(x), false, false);
  return __uint_as_float(t[0]) + __uint_as_float(t[1]);
}
__device__ __forceinline__ float xrow_max(float x) {
  auto s = __builtin_amdgcn_permlane16_swap(__float_as_uint(x), __float_as_uint(x), false, false);
  x = fmaxf(__uint_as_float(s[0]), __uint_as_float(s[1]));
  auto t = __builtin_amdgcn_permlane32_swap(__float_as_uint(x), __float_as_uint(x), false, false);
  return fmaxf(__uint_as_float(t[0]), __uint_as_float(t[1]));
}
__device__ __forceinline__ float wave_sum(float x) { return xrow_sum(row16_sum(x)); }
__device__ __forceinline__ float wave_max(float x) { return xrow_max(row16_max(x)); }
__device__ __forceinline__ u32x4 ldnt(const void* p) { return __builtin_nontemporal_load(reinterpret_cast<const u32x4*>(p)); }
__device__ __forceinline__ u32x4 ld16(const void* p) { return *reinterpret_cast<const u32x4*>(p); }

__device__ __forceinline__ void layernorm_row(const unsigned short* x, int lane, float* xn) {
  const unsigned* xw = reinterpret_cast<const unsigned*>(x);
  float v[10];
#pragma unroll
  for (int i = 0; i < 5; ++i) { unsigned u = xw[lane + 64 * i]; v[2 * i] = bflo(u); v[2 * i + 1] = bfhi(u); }
  float s = 0.f;
#pragma unroll
  for (int i = 0; i < 10; ++i) s += v[i];
  s = wave_sum(s);
  const float mean = s * (1.f / D_);
  float q = 0.f;
#pragma unroll
  for (int i = 0; i < 10; ++i) { const float d = v[i] - mean; q += d * d; }
  q = wave_sum(q);
  const float var = q * (1.f / D_);
  const float mb = rbf(mean), vb = rbf(var);
  const float den = rbf(sqrtf(rbf(vb + 1e-5f)));
#pragma unroll
  for (int i = 0; i < 10; ++i) v[i] = rbf(rbf(v[i] - mb) / den);
#pragma unroll
  for (int i = 0; i < 5; ++i) { *reinterpret_cast<float2*>(xn + 2 * (lane + 64 * i)) = make_float2(v[2 * i], v[2 * i + 1]); }
}

struct Args {
  const unsigned short *x, *k, *v, *wq, *wo, *wg, *wu, *wd;
  float* ws;
  unsigned short* out;
};
constexpr int WS_ML = 0, WS_Y = 1024, WS_YF = WS_Y + NATT * D_;

constexpr int RING = 16;

__global__ void __launch_bounds__(512, 2) kernA(Args a) {
  __shared__ __attribute__((aligned(16))) float lds[8192];
  const int tid = threadIdx.x, lane = tid & 63, w = __builtin_amdgcn_readfirstlane(tid >> 6), r = lane >> 4, c = lane & 15;
  const int bid = blockIdx.x;
  if (bid < NATT) {
    const int xcd = bid & 7, slot = bid >> 3;
    const int h = xcd * 4 + (slot >> 3), b = (slot & 7) >> 1, half = slot & 1;
    const size_t kvoff = ((size_t)(b * H_ + h) * SK + half * 2048 + w * 256) * DH + lane * 8;
    const unsigned short* kp = a.k + kvoff;
    const unsigned short* vp = a.v + kvoff;
    float* xn = lds;
    float* qpart = lds + 640;
    u32x4 ring[RING];
    {
      const unsigned short* wp = a.wq + (size_t)(80 * w + r) * (H_ * DH) + h * DH + 8 * c;
      u32x4 wv[20];
#pragma unroll
      for (int i = 0; i < 20; ++i) wv[i] = ld16(wp + (size_t)4 * i * (H_ * DH));
      layernorm_row(a.x + b * D_, lane, xn);
      float acc[8];
#pragma unroll
      for (int j = 0; j < 8; ++j) acc[j] = 0.f;
#pragma unroll
      for (int i = 0; i < 20; ++i) {
        const float xv = xn[80 * w + 4 * i + r];
#pragma unroll
        for (int j = 0; j < 4; ++j) { acc[2 * j] = fmaf(xv, bflo(wv[i][j]), acc[2 * j]); acc[2 * j + 1] = fmaf(xv, bfhi(wv[i][j]), acc[2 * j + 1]); }
      }
#pragma unroll
      for (int i = 0; i < RING; ++i) ring[i] = ldnt(kp + i * 512);
#pragma unroll
      for (int j = 0; j < 8; ++j) acc[j] = xrow_sum(acc[j]);
      if (r == 0) {
        *reinterpret_cast<f32x4*>(qpart + w * 128 + 8 * c) = f32x4{acc[0], acc[1], acc[2], acc[3]};
        *reinterpret_cast<f32x4*>(qpart + w * 128 + 8 * c + 4) = f32x4{acc[4], acc[5], acc[6], acc[7]};
      }
    }
    __syncthreads();
    unsigned qpk[4];
    {
      f32x4 q0 = {0, 0, 0, 0}, q1 = {0, 0, 0, 0};
#pragma unroll
      for (int ww = 0; ww < 8; ++ww) {
        q0 += *reinterpret_cast<const f32x4*>(qpart + ww * 128 + 8 * c);
        q1 += *reinterpret_cast<const f32x4*>(qpart + ww * 128 + 8 * c + 4);
      }
      qpk[0] = cvtpk(q0[0], q0[1]); qpk[1] = cvtpk(q0[2], q0[3]); qpk[2] = cvtpk(q1[0], q1[1]); qpk[3] = cvtpk(q1[2], q1[3]);
    }
    float s[4];
#pragma unroll
    for (int g = 0; g < 4; ++g) {
      float sg = 0.f;
#pragma unroll
      for (int j = 0; j < 16; ++j) {
        const u32x4 kv = ring[j];
        const int nxt = g * 16 + j + RING;
        ring[j] = (nxt < 64) ? ldnt(kp + nxt * 512) : ldnt(vp + (nxt - 64) * 512);
        float d = 0.f;
#pragma unroll
        for (int t = 0; t < 4; ++t) d = dot2(kv[t], qpk[t], d);
        d = row16_sum(d);
        sg = (c == j) ? d : sg;
      }
      s[g] = sg;
    }
    constexpr float SCALE = 0.08838834764831845f, LOG2E = 1.4426950408889634f;
#pragma unroll
    for (int g = 0; g < 4; ++g) {
      s[g] = rbf(rbf(s[g]) * SCALE);
    }
    const float m = wave_max(fmaxf(fmaxf(s[0], s[1]), fmaxf(s[2], s[3])));
    float p[4];
#pragma unroll
    for (int g = 0; g < 4; ++g) p[g] = __builtin_amdgcn_exp2f((s[g] - m) * LOG2E);
    const float l = wave_sum(p[0] + p[1] + p[2] + p[3]);
    float acc[8];
#pragma unroll
    for (int j = 0; j < 8; ++j) acc[j] = 0.f;
#define VSTEP(J) { const u32x4 vv = ring[J]; const int nxt = g * 16 + J + RING; if (nxt < 64) ring[J] = ldnt(vp + nxt * 512); \
      const float pb = dpp<0x150 + J>(p[g]); \
      _Pragma("unroll") for (int t = 0; t < 4; ++t) { acc[2 * t] = fmaf(pb, bflo(vv[t]), acc[2 * t]); acc[2 * t + 1] = fmaf(pb, bfhi(vv[t]), acc[2 * t + 1]); } }
#pragma unroll
    for (int g = 0; g < 4; ++g) {
      VSTEP(0) VSTEP(1) VSTEP(2) VSTEP(3) VSTEP(4) VSTEP(5) VSTEP(6) VSTEP(7)
      VSTEP(8) VSTEP(9) VSTEP(10) VSTEP(11) VSTEP(12) VSTEP(13) VSTEP(14) VSTEP(15)
    }
    const int rg = lane / 10, cg = lane - rg * 10;
    u32x4 wv[22];
    {
      const unsigned short* wp = a.wo + (size_t)(h * DH) * D_ + 80 * w + 8 * cg;
#pragma unroll
      for (int i = 0; i < 22; ++i) { int row = rg + 6 * i; row = row < 128 ? row : 127; wv[i] = ld16(wp + (size_t)row * D_); }
    }
    float* opart = lds + 2048;
    float* ml = lds + 2048 + 1024;
    float* ofin = lds + 2048 + 1024 + 32;
    float* ypart = lds + 4096;
#pragma unroll
    for (int j = 0; j < 8; ++j) acc[j] = xrow_sum(acc[j]);
    if (r == 0) {
      *reinterpret_cast<f32x4*>(opart + w * 128 + 8 * c) = f32x4{acc[0], acc[1], acc[2], acc[3]};
      *reinterpret_cast<f32x4*>(opart + w * 128 + 8 * c + 4) = f32x4{acc[4], acc[5], acc[6], acc[7]};
      if (c == 0) { ml[2 * w] = m; ml[2 * w + 1] = l; }
    }
    __syncthreads();
    if (tid < 144) {
      float M = ml[0];
#pragma unroll
      for (int ww = 1; ww < 8; ++ww) M = fmaxf(M, ml[2 * ww]);
      float o = 0.f, L = 0.f;
      const int d = tid < 128 ? tid : 0;
#pragma unroll
      for (int ww = 0; ww < 8; ++ww) { const float e = __builtin_amdgcn_exp2f((ml[2 * ww] - M) * LOG2E); o = fmaf(e, opart[ww * 128 + d], o); L = fmaf(e, ml[2 * ww + 1], L); }
      ofin[(tid % 6) * 24 + tid / 6] = tid < 128 ? o : 0.f;
      if (tid == 0) { a.ws[WS_ML + 2 * bid] = M; a.ws[WS_ML + 2 * bid + 1] = L; }
    }
    __syncthreads();
    if (lane < 60) {
      float y[8];
#pragma unroll
      for (int j = 0; j < 8; ++j) y[j] = 0.f;
#pragma unroll
      for (int i = 0; i < 22; ++i) {
        const float ov = ofin[rg * 24 + i];
#pragma unroll
        for (int t = 0; t < 4; ++t) { y[2 * t] = fmaf(ov, bflo(wv[i][t]), y[2 * t]); y[2 * t + 1] = fmaf(ov, bfhi(wv[i][t]), y[2 * t + 1]); }
      }
      *reinterpret_cast<f32x4*>(ypart + (w * 6 + rg) * 80 + 8 * cg) = f32x4{y[0], y[1], y[2], y[3]};
      *reinterpret_cast<f32x4*>(ypart + (w * 6 + rg) * 80 + 8 * cg + 4) = f32x4{y[4], y[5], y[6], y[7]};
    }
    __syncthreads();
    if (lane < 40) {
      float y0 = 0.f, y1 = 0.f;
#pragma unroll
      for (int g = 0; g < 6; ++g) { const float2 t = *reinterpret_cast<const float2*>(ypart + (w * 6 + g) * 80 + 2 * lane); y0 += t.x; y1 += t.y; }
      *reinterpret_cast<float2*>(a.ws + WS_Y + (size_t)bid * D_ + 80 * w + 2 * lane) = make_float2(y0, y1);
    }
  } else {
    const int j = bid - NATT;
    float* xn = lds;
    float* gpart = lds + 2560;
    float* hh = lds + 2560 + 4096;
    const int r8 = lane >> 3, c8 = lane & 7;
    u32x4 gv[10], uv[10];
    const size_t off = (size_t)(80 * w + r8) * F_ + 64 * j + 8 * c8;
#pragma unroll
    for (int i = 0; i < 10; ++i) { gv[i] = ld16(a.wg + off + (size_t)8 * i * F_); uv[i] = ld16(a.wu + off + (size_t)8 * i * F_); }
    if (w < 4) layernorm_row(a.x + w * D_, lane, xn + w * D_);
    __syncthreads();
#pragma unroll
    for (int bb = 0; bb < 4; ++bb) {
      float ga[8], ua[8];
#pragma unroll
      for (int t = 0; t < 8; ++t) { ga[t] = 0.f; ua[t] = 0.f; }
#pragma unroll
      for (int i = 0; i < 10; ++i) {
        const float xv = xn[bb * D_ + 80 * w + 8 * i + r8];
#pragma unroll
        for (int t = 0; t < 4; ++t) {
          ga[2 * t] = fmaf(xv, bflo(gv[i][t]), ga[2 * t]); ga[2 * t + 1] = fmaf(xv, bfhi(gv[i][t]), ga[2 * t + 1]);
          ua[2 * t] = fmaf(xv, bflo(uv[i][t]), ua[2 * t]); ua[2 * t + 1] = fmaf(xv, bfhi(uv[i][t]), ua[2 * t + 1]);
        }
      }
#pragma unroll
      for (int t = 0; t < 8; ++t) { ga[t] += dpp<XOR8>(ga[t]); ga[t] = xrow_sum(ga[t]); ua[t] += dpp<XOR8>(ua[t]); ua[t] = xrow_sum(ua[t]); }
      if (lane < 8) {
        float* gp = gpart + ((w * 2 + 0) * 4 + bb) * 64 + 8 * c8;
        float* up = gpart + ((w * 2 + 1) * 4 + bb) * 64 + 8 * c8;
#pragma unroll
        for (int t = 0; t < 8; ++t) { gp[t] = ga[t]; up[t] = ua[t]; }
      }
    }
    const int rg = lane / 10, cg = lane - rg * 10;
    u32x4 wv[11];
    {
      const unsigned short* wp = a.wd + (size_t)(64 * j) * D_ + 80 * w + 8 * cg;
#pragma unroll
      for (int i = 0; i < 11; ++i) { int row = rg + 6 * i; row = row < 64 ? row : 63; wv[i] = ld16(wp + (size_t)row * D_); }
    }
    __syncthreads();
    if (tid < 256) {
      float g = 0.f, u = 0.f;
#pragma unroll
      for (int ww = 0; ww < 8; ++ww) { g += gpart[(ww * 2 + 0) * 256 + tid]; u += gpart[(ww * 2 + 1) * 256 + tid]; }
      g = rbf(g); u = rbf(u);
      const float sg = rbf(g / (1.f + __expf(-g)));
      hh[tid] = rbf(sg * u);
    }
    __syncthreads();
    float* ypart = lds;
#pragma unroll
    for (int bb = 0; bb < 4; ++bb) {
      if (lane < 60) {
        float y[8];
#pragma unroll
        for (int t = 0; t < 8; ++t) y[t] = 0.f;
#pragma unroll
        for (int i = 0; i < 11; ++i) {
          const int row = rg + 6 * i;
          const float ov = row < 64 ? hh[bb * 64 + row] : 0.f;
#pragma unroll
          for (int t = 0; t < 4; ++t) { y[2 * t] = fmaf(ov, bflo(wv[i][t]), y[2 * t]); y[2 * t + 1] = fmaf(ov, bfhi(wv[i][t]), y[2 * t + 1]); }
        }
        *reinterpret_cast<f32x4*>(ypart + (w * 6 + rg) * 80 + 8 * cg) = f32x4{y[0], y[1], y[2], y[3]};
        *reinterpret_cast<f32x4*>(ypart + (w * 6 + rg) * 80 + 8 * cg + 4) = f32x4{y[4], y[5], y[6], y[7]};
      }
      __syncthreads();
      if (lane < 40) {
        float y0 = 0.f, y1 = 0.f;
#pragma unroll
        for (int g = 0; g < 6; ++g) { const float2 t = *reinterpret_cast<const float2*>(ypart + (w * 6 + g) * 80 + 2 * lane); y0 += t.x; y1 += t.y; }
        *reinterpret_cast<float2*>(a.ws + WS_YF + (size_t)(j * 4 + bb) * D_ + 80 * w + 2 * lane) = make_float2(y0, y1);
      }
      __syncthreads();
    }
  }
}

__global__ void __launch_bounds__(256) kernB(Args a) {
  __shared__ __attribute__((aligned(16))) float red[4][16];
  const int tid = threadIdx.x, lane = tid & 63, w = tid >> 6;
  const int b = blockIdx.x / 40, cb = (blockIdx.x % 40) * 16;
  const int p = tid >> 2, c4 = tid & 3;
  const int h = p >> 1, half = p & 1;
  const int bid = ((h & 3) * 8 + b * 2 + half) * 8 + (h >> 2);
  const int bidp = ((h & 3) * 8 + b * 2 + (half ^ 1)) * 8 + (h >> 2);
  const f32x4 y = *reinterpret_cast<const f32x4*>(a.ws + WS_Y + (size_t)bid * D_ + cb + 4 * c4);
  const float2 ml = *reinterpret_cast<const float2*>(a.ws + WS_ML + 2 * bid);
  const float2 mlp = *reinterpret_cast<const float2*>(a.ws + WS_ML + 2 * bidp);
  f32x4 yf = {0, 0, 0, 0};
  if (p < NFFN) yf = *reinterpret_cast<const f32x4*>(a.ws + WS_YF + (size_t)(p * 4 + b) * D_ + cb + 4 * c4);
  constexpr float LOG2E = 1.4426950408889634f;
  const float M = fmaxf(ml.x, mlp.x);
  const float e = __builtin_amdgcn_exp2f((ml.x - M) * LOG2E), ep = __builtin_amdgcn_exp2f((mlp.x - M) * LOG2E);
  const float wgt = e / (e * ml.y + ep * mlp.y);
  float va[4], vf[4];
#pragma unroll
  for (int t = 0; t < 4; ++t) {
    va[t] = y[t] * wgt; vf[t] = yf[t];
    va[t] += dpp<0x124>(va[t]); va[t] += dpp<XOR8>(va[t]); va[t] = xrow_sum(va[t]);
    vf[t] += dpp<0x124>(vf[t]); vf[t] += dpp<XOR8>(vf[t]); vf[t] = xrow_sum(vf[t]);
  }
  __shared__ __attribute__((aligned(16))) float redf[4][16];
  if (lane < 4) {
#pragma unroll
    for (int t = 0; t < 4; ++t) { red[w][4 * lane + t] = va[t]; redf[w][4 * lane + t] = vf[t]; }
  }
  __syncthreads();
  if (tid < 16) {
    float A = red[0][tid] + red[1][tid] + red[2][tid] + red[3][tid];
    float Bv = redf[0][tid] + redf[1][tid] + redf[2][tid] + redf[3][tid];
    const float xv = bflo(a.x[b * D_ + cb + tid]);
    const float o = rbf(xv + rbf(A)) + rbf(Bv);
    a.out[b * D_ + cb + tid] = (unsigned short)(cvtpk(o, o) & 0xffffu);
  }
}

extern "C" void kernel_launch(void* const* d_in, const int* in_sizes, int n_in, void* d_out, int out_size, void* d_ws, size_t ws_size,
                              hipStream_t stream) {
  Args a;
  a.x = (const unsigned short*)d_in[0]; a.k = (const unsigned short*)d_in[1]; a.v = (const unsigned short*)d_in[2];
  a.wq = (const unsigned short*)d_in[3]; a.wo = (const unsigned short*)d_in[4];
  a.wg = (const unsigned short*)d_in[6]; a.wu = (const unsigned short*)d_in[7]; a.wd = (const unsigned short*)d_in[8];
  a.ws = (float*)d_ws; a.out = (unsigned short*)d_out;
  kernA<<<NATT + NFFN, 512, 0, stream>>>(a);
  kernB<<<160, 256, 0, stream>>>(a);
}
```

```cpp
#include <hip/hip_runtime.h>
#include <stdint.h>

#define RING 8
#define THR 2
#define PRE 4

typedef unsigned short u16;
typedef unsigned u32x4 __attribute__((ext_vector_type(4)));
typedef unsigned u32x2 __attribute__((ext_vector_type(2)));
typedef float f32x4 __attribute__((ext_vector_type(4)));
typedef __bf16 bf16x2 __attribute__((ext_vector_type(2)));
typedef __bf16 bf16x8 __attribute__((ext_vector_type(8)));
typedef __amdgpu_buffer_rsrc_t rsrc_t;

constexpr int D_ = 640, H_ = 32, SK = 4096, DH = 128, F_ = 1728;
constexpr int LA = RING - 1;
constexpr int AUX_NT = 2, AUX_SC1 = 16;
constexpr unsigned WS_BLK = 1u << 20, Y_STRIDE = 8448, ML_OFF = 576u << 10, EP_OFF = 640u << 10;

__device__ __forceinline__ float dot2(unsigned a, unsigned b, float acc) {
  return __builtin_amdgcn_fdot2_f32_bf16(__builtin_bit_cast(bf16x2, a), __builtin_bit_cast(bf16x2, b), acc, false);
}
__device__ __forceinline__ float asf(unsigned u) { return __builtin_bit_cast(float, u); }
__device__ __forceinline__ unsigned asu(float f) { return __builtin_bit_cast(unsigned, f); }
__device__ __forceinline__ float bflo(unsigned u) { return asf(u << 16); }
__device__ __forceinline__ float bfhi(unsigned u) { return asf(u & 0xffff0000u); }
__device__ __forceinline__ unsigned cvtpk(float lo, float hi) {
  unsigned r; asm("v_cvt_pk_bf16_f32 %0, %1, %2" : "=v"(r) : "v"(lo), "v"(hi)); return r;
}
__device__ __forceinline__ float rbf(float x) { return bflo(cvtpk(x, x)); }
template <int CTRL> __device__ __forceinline__ float dpp(float x) {
  return asf(__builtin_amdgcn_mov_dpp(asu(x), CTRL, 0xf, 0xf, true));
}
constexpr int XOR1 = 0xB1, XOR2 = 0x4E, XOR7 = 0x141, ROR8 = 0x128, ROR4 = 0x124;
__device__ __forceinline__ float row16_sum(float x) {
  x += dpp<ROR8>(x); x += dpp<XOR7>(x); x += dpp<XOR2>(x); x += dpp<XOR1>(x); return x;
}
__device__ __forceinline__ float row16_max(float x) {
  x = fmaxf(x, dpp<ROR8>(x)); x = fmaxf(x, dpp<XOR7>(x)); x = fmaxf(x, dpp<XOR2>(x)); x = fmaxf(x, dpp<XOR1>(x)); return x;
}
__device__ __forceinline__ float tr32(float a, float b) {
  auto s = __builtin_amdgcn_permlane32_swap(asu(a), asu(b), false, false);
  return asf(s[0]) + asf(s[1]);
}
__device__ __forceinline__ float tr16(float a, float b) {
  auto s = __builtin_amdgcn_permlane16_swap(asu(a), asu(b), false, false);
  return asf(s[0]) + asf(s[1]);
}
__device__ __forceinline__ float xrow_sum(float x) { x = tr16(x, x); return tr32(x, x); }
__device__ __forceinline__ float xrow_max(float x) {
  auto s = __builtin_amdgcn_permlane16_swap(asu(x), asu(x), false, false);
  x = fmaxf(asf(s[0]), asf(s[1]));
  auto t = __builtin_amdgcn_permlane32_swap(asu(x), asu(x), false, false);
  return fmaxf(asf(t[0]), asf(t[1]));
}
__device__ __forceinline__ float wave_sum(float x) { return xrow_sum(row16_sum(x)); }
__device__ __forceinline__ float wave_max(float x) { return xrow_max(row16_max(x)); }
__device__ __forceinline__ void tr_reduce8(const float* v, float& o0, float& o1) {
  const float t0 = tr32(v[0], v[4]), t1 = tr32(v[1], v[5]), t2 = tr32(v[2], v[6]), t3 = tr32(v[3], v[7]);
  o0 = tr16(t0, t2); o1 = tr16(t1, t3);
}
__device__ __forceinline__ rsrc_t mk(const void* p, unsigned bytes) { return __builtin_amdgcn_make_buffer_rsrc((void*)p, 0, (int)bytes, 0x00020000); }
__device__ __forceinline__ u32x4 ld128(rsrc_t r, unsigned v, unsigned s, int) = delete;
template <int AUX> __device__ __forceinline__ u32x4 ldx4(rsrc_t r, unsigned v, unsigned s) { return __builtin_amdgcn_raw_buffer_load_b128(r, v, s, AUX); }
template <int AUX> __device__ __forceinline__ u32x2 ldx2(rsrc_t r, unsigned v, unsigned s) { return __builtin_amdgcn_raw_buffer_load_b64(r, v, s, AUX); }
__device__ __forceinline__ unsigned plo(unsigned a, unsigned b) { return __builtin_amdgcn_perm(b, a, 0x05040100u); }
__device__ __forceinline__ unsigned phi(unsigned a, unsigned b) { return __builtin_amdgcn_perm(b, a, 0x07060302u); }
__device__ __forceinline__ f32x4 mfma(u32x4 a, u32x4 b, f32x4 c) {
  return __builtin_amdgcn_mfma_f32_16x16x32_bf16(__builtin_bit_cast(bf16x8, a), __builtin_bit_cast(bf16x8, b), c, 0, 0, 0);
}
__device__ __forceinline__ void to_frags(const u32x2* L, u32x4* F) {
#pragma unroll
  for (int t = 0; t < 4; ++t) {
    F[0][t] = plo(L[2 * t][0], L[2 * t + 1][0]); F[1][t] = phi(L[2 * t][0], L[2 * t + 1][0]);
    F[2][t] = plo(L[2 * t][1], L[2 * t + 1][1]); F[3][t] = phi(L[2 * t][1], L[2 * t + 1][1]);
  }
}
template <int N> __device__ __forceinline__ void throttle() {
  if constexpr (N >= 0) asm volatile("s_waitcnt vmcnt(%0)" :: "n"(N) : "memory");
  else asm volatile("" ::: "memory");
}

struct Args {
  const u16 *x, *k, *v, *wq, *wo, *wg, *wu, *wd;
  unsigned char* ws;
  u16* out;
};
#define STAMP(i) do {} while (0)

constexpr float SCALE = 0.08838834764831845f, LOG2E = 1.4426950408889634f;

__global__ void __launch_bounds__(512, 2) fused_block(Args a) {
  __shared__ __attribute__((aligned(16))) unsigned xnb[320];
  __shared__ __attribute__((aligned(16))) float qpart[8 * 128];
  __shared__ __attribute__((aligned(16))) float fpart[8 * 32];
  __shared__ __attribute__((aligned(16))) unsigned qw[8 * 64];
  __shared__ __attribute__((aligned(16))) u16 hw[8 * 32];
  __shared__ __attribute__((aligned(16))) unsigned alds[16 * 68];
  __shared__ __attribute__((aligned(16))) float red[8 * 16];
  STAMP(0);
  const int tid = threadIdx.x, lane = tid & 63, w = __builtin_amdgcn_readfirstlane(tid >> 6), r = lane >> 4, c = lane & 15;
  const int bid = blockIdx.x, xcd = bid & 7, slot = bid >> 3;
  const int h = xcd * 4 + (slot >> 3), b = (slot & 7) >> 1, half = slot & 1, u = h * 2 + half;
  const int ul = u & 7;
  const int g0 = 27 * xcd + (ul < 3 ? 4 * ul : 3 * ul + 3), gcnt = ul < 3 ? 4 : 3;

  const size_t kvo = ((size_t)(b * H_ + h) * SK + half * 2048) * DH;
  const rsrc_t rK = mk(a.k + kvo, 524288), rV = mk(a.v + kvo, 524288);
  const rsrc_t rWS = mk(a.ws + (size_t)b * WS_BLK, WS_BLK);
  const unsigned vl16 = lane * 16;
  const int rot = bid;
  auto soff = [&](int i) -> unsigned { return (unsigned)(((i + rot) & 63) * 8192 + w * 1024); };

  unsigned xr[5];
  {
    const unsigned* xw = reinterpret_cast<const unsigned*>(a.x + b * D_);
#pragma unroll
    for (int i = 0; i < 5; ++i) xr[i] = xw[lane + 64 * i];
  }
  const int ocol = u < 32 ? 16 * u + (tid & 15) : 512 + 4 * (u - 32) + (tid & 3);
  const unsigned xres = a.x[b * D_ + ocol];
  const unsigned ep = __builtin_amdgcn_raw_buffer_load_b32(rWS, 0, EP_OFF, AUX_SC1);
  asm volatile("" ::: "memory");
  u32x4 ring[RING];
#pragma unroll
  for (int i = 0; i < PRE; ++i) ring[i] = ldx4<AUX_NT>(rK, vl16, soff(i));
  asm volatile("" ::: "memory");
  u32x4 wqv[20];
  {
    const rsrc_t rQ = mk(a.wq + h * DH, D_ * 8192 - h * 256);
    const unsigned vo = r * 16384 + c * 16;
#pragma unroll
    for (int i = 0; i < 20; ++i) wqv[i] = ldx4<AUX_NT>(rQ, vo, (unsigned)(80 * w + 8 * (i >> 1) + (i & 1)) * 8192u);
  }
  asm volatile("" ::: "memory");
#pragma unroll
  for (int i = PRE; i < LA; ++i) ring[i] = ldx4<AUX_NT>(rK, vl16, soff(i));
  asm volatile("" ::: "memory");
  u32x4 fv[10];
  const int r16 = lane >> 2;
  {
    const rsrc_t rF = mk(w < 4 ? a.wg : a.wu, D_ * F_ * 2);
    const int g4 = (lane & 3) < gcnt ? (lane & 3) : gcnt - 1;
    const unsigned vo = r16 * (2 * F_ * 2) + (g0 + g4) * 16;
#pragma unroll
    for (int i = 0; i < 10; ++i) fv[i] = ldx4<AUX_NT>(rF, vo, (unsigned)(160 * (w & 3) + 32 * (i >> 1) + (i & 1)) * (F_ * 2));
  }
  asm volatile("" ::: "memory");
  {
    float v[10];
#pragma unroll
    for (int i = 0; i < 5; ++i) { v[2 * i] = bflo(xr[i]); v[2 * i + 1] = bfhi(xr[i]); }
    float s = 0.f;
#pragma unroll
    for (int i = 0; i < 10; ++i) s += v[i];
    const float mean = wave_sum(s) * (1.f / D_);
    float q = 0.f;
#pragma unroll
    for (int i = 0; i < 10; ++i) { const float d = v[i] - mean; q += d * d; }
    const float var = wave_sum(q) * (1.f / D_);
    const float mb = rbf(mean), den = rbf(sqrtf(rbf(rbf(var) + 1e-5f)));
#pragma unroll
    for (int i = 0; i < 5; ++i) xnb[lane + 64 * i] = cvtpk(rbf(v[2 * i] - mb) / den, rbf(v[2 * i + 1] - mb) / den);
  }
  STAMP(1);
  {
    float acc[8];
#pragma unroll
    for (int j = 0; j < 8; ++j) acc[j] = 0.f;
#pragma unroll
    for (int t = 0; t < 10; ++t) {
      const unsigned xp = xnb[40 * w + 4 * t + r];
#pragma unroll
      for (int j = 0; j < 4; ++j) {
        acc[2 * j] = dot2(plo(wqv[2 * t][j], wqv[2 * t + 1][j]), xp, acc[2 * j]);
        acc[2 * j + 1] = dot2(phi(wqv[2 * t][j], wqv[2 * t + 1][j]), xp, acc[2 * j + 1]);
      }
    }
    float o0, o1; tr_reduce8(acc, o0, o1);
    *reinterpret_cast<float2*>(qpart + w * 128 + 8 * c + 2 * r) = make_float2(o0, o1);
  }
  STAMP(2);
  {
    float acc[8];
#pragma unroll
    for (int j = 0; j < 8; ++j) acc[j] = 0.f;
#pragma unroll
    for (int t = 0; t < 5; ++t) {
      const unsigned xp = xnb[80 * (w & 3) + 16 * t + r16];
#pragma unroll
      for (int j = 0; j < 4; ++j) {
        acc[2 * j] = dot2(plo(fv[2 * t][j], fv[2 * t + 1][j]), xp, acc[2 * j]);
        acc[2 * j + 1] = dot2(phi(fv[2 * t][j], fv[2 * t + 1][j]), xp, acc[2 * j + 1]);
      }
    }
    float o0, o1; tr_reduce8(acc, o0, o1);
    o0 += dpp<ROR4>(o0); o0 += dpp<ROR8>(o0); o1 += dpp<ROR4>(o1); o1 += dpp<ROR8>(o1);
    if (c < 4) *reinterpret_cast<float2*>(fpart + w * 32 + 8 * c + 2 * r) = make_float2(o0, o1);
  }
  STAMP(3);
  __syncthreads();
  u32x4 qv;
  {
    float q0 = 0.f, q1 = 0.f;
#pragma unroll
    for (int ww = 0; ww < 8; ++ww) { const float2 t = *reinterpret_cast<const float2*>(qpart + ww * 128 + 2 * lane); q0 += t.x; q1 += t.y; }
    qw[w * 64 + lane] = cvtpk(q0, q1);
    qv = *reinterpret_cast<const u32x4*>(qw + w * 64 + 4 * c);
  }
  const unsigned tag = __builtin_amdgcn_readfirstlane(ep) + 1u;
  STAMP(4);

#define KCOMP(J, SG) { const u32x4 kv = ring[(J) % RING]; float d = dot2(kv[0], qv[0], 0.f); d = dot2(kv[1], qv[1], d); d = dot2(kv[2], qv[2], d); d = dot2(kv[3], qv[3], d); \
    d = row16_sum(d); SG = (c == (J)) ? d : SG; }
  float s0 = 0.f, s1 = 0.f, s2 = 0.f, s3 = 0.f;
  u32x4 bo[5][4];
  float yf[8];
  {
    const rsrc_t rO = mk(a.wo + (size_t)h * DH * D_, DH * D_ * 2);
    const rsrc_t rD = mk(a.wd + (size_t)g0 * 8 * D_, gcnt * 8 * D_ * 2);
    const rsrc_t rDx = mk(a.wd + (size_t)g0 * 8 * D_, (w & 3) == 0 ? gcnt * 8 * D_ * 2 : 0);
    const unsigned vo = r * (8 * D_ * 2) + (64 * w + 4 * c) * 2, vx = r * (8 * D_ * 2) + (512 + 64 * (w >> 2) + 4 * c) * 2;
    u32x2 L[7][8];
#pragma unroll
    for (int j = 0; j < 16; ++j) {
      asm volatile("" ::: "memory");
      ring[(j + LA) % RING] = ldx4<AUX_NT>(rK, vl16, soff(j + LA));
      if (j < 14) {
#pragma unroll
        for (int i = 4 * (j & 1); i < 4 * (j & 1) + 4; ++i) {
          const int un = j >> 1;
          if (un == 0) L[0][i] = ldx2<AUX_NT>(rD, vo + i * (D_ * 2), 0);
          else if (un == 1) L[1][i] = ldx2<AUX_NT>(rDx, vx + i * (D_ * 2), 0);
          else if (un < 6) L[un][i] = ldx2<AUX_NT>(rO, vo + i * (D_ * 2), (un - 2) * (32 * D_ * 2));
          else L[6][i] = ldx2<AUX_NT>(rO, vx + i * (D_ * 2), (w & 3) * (32 * D_ * 2));
        }
      }
      asm volatile("" ::: "memory");
      KCOMP(j, s3)
    }
    {
      float g = 0.f;
#pragma unroll
      for (int ww = 0; ww < 4; ++ww) g += fpart[((lane >> 5) * 4 + ww) * 32 + (lane & 31)];
      g = rbf(g);
      auto sw = __builtin_amdgcn_permlane32_swap(asu(g), asu(g), false, false);
      const float gg = asf(sw[0]), uu = asf(sw[1]);
      const float sg = rbf(gg / (1.f + __expf(-gg)));
      const float hv = (lane & 31) < 8 * gcnt ? sg * uu : 0.f;
      if (lane < 32) hw[w * 32 + lane] = (u16)cvtpk(hv, hv);
      u32x4 ah = *reinterpret_cast<const u32x4*>(hw + w * 32 + 8 * r);
      if (c != 0) ah = u32x4{0u, 0u, 0u, 0u};
      u32x4 F[4];
      const f32x4 z = {0.f, 0.f, 0.f, 0.f};
      to_frags(L[0], F);
#pragma unroll
      for (int f = 0; f < 4; ++f) yf[f] = mfma(ah, F[f], z)[0];
      to_frags(L[1], F);
#pragma unroll
      for (int f = 0; f < 4; ++f) yf[4 + f] = mfma(ah, F[f], z)[0];
    }
#pragma unroll
    for (int un = 0; un < 5; ++un) to_frags(L[2 + un], bo[un]);
  }
  STAMP(5);
#pragma unroll 1
  for (int g = 1; g < 4; ++g) {
    s0 = s1; s1 = s2; s2 = s3;
    const rsrc_t rN = g == 3 ? rV : rK;
#pragma unroll
    for (int j = 0; j < 16; ++j) {
      throttle<THR>();
      ring[(j + LA) % RING] = ldx4<AUX_NT>(j + LA < 16 ? rK : rN, vl16, soff(g * 16 + j + LA));
      asm volatile("" ::: "memory");
      KCOMP(j, s3)
    }
  }
  STAMP(6);
  s0 = rbf(rbf(s0) * SCALE); s1 = rbf(rbf(s1) * SCALE); s2 = rbf(rbf(s2) * SCALE); s3 = rbf(rbf(s3) * SCALE);
  const float mw = wave_max(fmaxf(fmaxf(s0, s1), fmaxf(s2, s3)));
  float p0 = __builtin_amdgcn_exp2f((s0 - mw) * LOG2E), p1 = __builtin_amdgcn_exp2f((s1 - mw) * LOG2E);
  float p2 = __builtin_amdgcn_exp2f((s2 - mw) * LOG2E), p3 = __builtin_amdgcn_exp2f((s3 - mw) * LOG2E);
  {
    const float lw = wave_sum((p0 + p1) + (p2 + p3));
    if (lane == 0) __builtin_amdgcn_raw_buffer_store_b128(u32x4{asu(mw), tag, asu(lw), tag}, rWS, 0, ML_OFF + h * 256 + (half * 8 + w) * 16, AUX_SC1);
  }
  float acc[8];
#pragma unroll
  for (int j = 0; j < 8; ++j) acc[j] = 0.f;
  float scale = 0.f;
  bool mlok = false;
  auto mlcalc = [&](u32x4 ml) {
    mlok = __all(ml[1] == tag && ml[3] == tag);
    const float mi = asf(ml[0]), M = row16_max(mi);
    const float L = row16_sum(__builtin_amdgcn_exp2f((mi - M) * LOG2E) * asf(ml[2]));
    scale = __builtin_amdgcn_exp2f((mw - M) * LOG2E) / L;
  };
  const rsrc_t rZ = mk(a.v, 0);
#pragma unroll 1
  for (int g = 0; g < 4; ++g) {
    const rsrc_t rN = g == 3 ? rZ : rV;
    asm volatile("" ::: "memory");
    const u32x4 ml = ldx4<AUX_SC1>(rWS, c * 16, ML_OFF + h * 256);
#define VSTEP(J) { throttle<THR>(); \
      ring[((J) + LA) % RING] = ldx4<AUX_NT>((J) + LA < 16 ? rV : rN, vl16, soff(g * 16 + (J) + LA)); \
      asm volatile("" ::: "memory"); \
      const u32x4 vv = ring[(J) % RING]; const float pb = dpp<0x150 + (J)>(p0); \
      _Pragma("unroll") for (int t = 0; t < 4; ++t) { acc[2 * t] = fmaf(pb, bflo(vv[t]), acc[2 * t]); acc[2 * t + 1] = fmaf(pb, bfhi(vv[t]), acc[2 * t + 1]); } }
    VSTEP(0) VSTEP(1) VSTEP(2) VSTEP(3) VSTEP(4) VSTEP(5) VSTEP(6) VSTEP(7)
    mlcalc(ml);
    VSTEP(8) VSTEP(9) VSTEP(10) VSTEP(11) VSTEP(12) VSTEP(13) VSTEP(14) VSTEP(15)
    p0 = p1; p1 = p2; p2 = p3;
  }
  STAMP(7);
  for (int it = 0; !mlok && it < (1 << 18); ++it) {
    asm volatile("" ::: "memory");
    __builtin_amdgcn_s_sleep(2);
    mlcalc(ldx4<AUX_SC1>(rWS, c * 16, ML_OFF + h * 256));
  }
  {
    float o0, o1; tr_reduce8(acc, o0, o1);
    o0 *= scale; o1 *= scale;
    const unsigned hi = cvtpk(o0, o1);
    alds[w * 68 + 4 * c + r] = hi;
    alds[(8 + w) * 68 + 4 * c + r] = cvtpk(o0 - bflo(hi), o1 - bfhi(hi));
  }
  __syncthreads();
  STAMP(8);
  {
    u32x4 af[5];
#pragma unroll
    for (int s = 0; s < 4; ++s) af[s] = *reinterpret_cast<const u32x4*>(alds + c * 68 + 16 * s + 4 * r);
    af[4] = *reinterpret_cast<const u32x4*>(alds + c * 68 + 16 * (w & 3) + 4 * r);
    f32x4 C[8];
#pragma unroll
    for (int f = 0; f < 8; ++f) C[f] = f32x4{yf[f], 0.f, 0.f, 0.f};
#pragma unroll
    for (int s = 0; s < 4; ++s)
#pragma unroll
      for (int f = 0; f < 4; ++f) C[f] = mfma(af[s], bo[s][f], C[f]);
#pragma unroll
    for (int f = 0; f < 4; ++f) C[4 + f] = mfma(af[4], bo[4][f], C[4 + f]);
    float z[8];
#pragma unroll
    for (int f = 0; f < 8; ++f) z[f] = (C[f][0] + C[f][1]) + (C[f][2] + C[f][3]);
    float y0, y1; tr_reduce8(z, y0, y1);
    const unsigned gi = r < 2 ? 64 * w + 4 * c + 2 * r : 512 + ((w >> 2) * 16 + c) * 16 + (w & 3) * 4 + 2 * (r - 2);
    __builtin_amdgcn_raw_buffer_store_b128(u32x4{asu(y0), tag, asu(y1), tag}, rWS, gi * 8, u * Y_STRIDE, AUX_SC1);
  }
  STAMP(9);
  {
    const unsigned vo = (tid >> 3) * Y_STRIDE + (tid & 7) * 16;
    float y0 = 0.f, y1 = 0.f;
    bool ok = false;
    for (int it = 0; it < (1 << 18); ++it) {
      asm volatile("" ::: "memory");
      if (!ok) {
        const u32x4 t = ldx4<AUX_SC1>(rWS, vo, u * 128);
        ok = t[1] == tag && t[3] == tag;
        y0 = asf(t[0]); y1 = asf(t[2]);
      }
      if (__all(ok)) break;
      __builtin_amdgcn_s_sleep(4);
    }
    float y = tr32(y0, y1);
    y = tr16(y, y);
    y += dpp<ROR8>(y);
    if ((lane & 24) == 0) red[w * 16 + 2 * (lane & 7) + (lane >> 5)] = y;
  }
  STAMP(10);
  __syncthreads();
  if (tid < 16) {
    float y = 0.f;
#pragma unroll
    for (int ww = 0; ww < 8; ++ww) y += red[ww * 16 + tid];
    if (u >= 32) { y += dpp<ROR4>(y); y += dpp<ROR8>(y); }
    if (u < 32 || tid < 4) a.out[b * D_ + ocol] = (u16)cvtpk(bflo(xres) + y, 0.f);
    if (u == 0 && tid == 0) __builtin_amdgcn_raw_buffer_store_b32(tag, rWS, 0, EP_OFF, AUX_SC1);
  }
}

extern "C" void kernel_launch(void* const* d_in, const int* in_sizes, int n_in, void* d_out, int out_size, void* d_ws, size_t ws_size,
                              hipStream_t stream) {
  Args a;
  a.x = (const u16*)d_in[0]; a.k = (const u16*)d_in[1]; a.v = (const u16*)d_in[2];
  a.wq = (const u16*)d_in[3]; a.wo = (const u16*)d_in[4];
  a.wg = (const u16*)d_in[6]; a.wu = (const u16*)d_in[7]; a.wd = (const u16*)d_in[8];
  a.ws = (unsigned char*)d_ws; a.out = (u16*)d_out;
  fused_block<<<256, 512, 0, stream>>>(a);
}
```

```cpp
#include <hip/hip_runtime.h>
#include <stdint.h>

#define DK 4
#define VDEEP 6
#define KDEEP 16
#define PA 3
#define PB 4
#define PX 0
#define NP 10
#define FS 15
#define YSLEEP 2
#define FSLEEP 4

#define DL 2
#define DH2 4
#define TA 250
#define TAU 228
#define GE 1
#define PFC 13312
#define PFS 0
#define PFK 0
typedef unsigned short u16;
typedef unsigned u32x4 __attribute__((ext_vector_type(4)));
typedef unsigned u32x2 __attribute__((ext_vector_type(2)));
typedef float f32x4 __attribute__((ext_vector_type(4)));
typedef float f32x2 __attribute__((ext_vector_type(2)));
typedef __bf16 bf16x2 __attribute__((ext_vector_type(2)));
typedef __bf16 bf16x8 __attribute__((ext_vector_type(8)));
typedef __amdgpu_buffer_rsrc_t rsrc_t;

constexpr int D_ = 640, H_ = 32, SK = 4096, DH = 128, F_ = 1728;
constexpr int AUX_NT = 2, AUX_SC1 = 16;
#define AUXK 2
#define AUXV 2
#define AUXP 2
#define AUXL 16
#define AUXS 16
#define AUXQL 16
#define AUXQS 16
#define RQG rWS
#define QTAG tag
#define QTG tg
#define RY rWS
#define YTAG tag
#define AUXW 0
constexpr unsigned Q_OFF = 0, G_OFF = 1u << 20, EP_OFF = (2u << 20) - 4096, Y_OFF = 2u << 20, Y_BLK = 1u << 20, Y_STRIDE = 4224, ML_OFF = 576u << 10, FL_OFF = 592u << 10, GROW = 864;
constexpr unsigned WS_USED = 6u << 20;

__device__ __forceinline__ float dot2(unsigned a, unsigned b, float acc) {
  return __builtin_amdgcn_fdot2_f32_bf16(__builtin_bit_cast(bf16x2, a), __builtin_bit_cast(bf16x2, b), acc, false);
}
__device__ __forceinline__ float asf(unsigned u) { return __builtin_bit_cast(float, u); }
__device__ __forceinline__ unsigned asu(float f) { return __builtin_bit_cast(unsigned, f); }
__device__ __forceinline__ float bflo(unsigned u) { return asf(u << 16); }
__device__ __forceinline__ float bfhi(unsigned u) { return asf(u & 0xffff0000u); }
__device__ __forceinline__ unsigned cvtpk(float lo, float hi) {
  return __builtin_bit_cast(unsigned, __builtin_convertvector(f32x2{lo, hi}, bf16x2));
}
__device__ __forceinline__ float rbf(float x) { return bflo(cvtpk(x, x)); }
template <int CTRL> __device__ __forceinline__ float dpp(float x) {
  return asf(__builtin_amdgcn_mov_dpp(asu(x), CTRL, 0xf, 0xf, true));
}
constexpr int XOR1 = 0xB1, XOR2 = 0x4E, XOR7 = 0x141, ROR8 = 0x128, ROR4 = 0x124;
__device__ __forceinline__ float row16_sum(float x) {
  x += dpp<ROR8>(x); x += dpp<XOR7>(x); x += dpp<XOR2>(x); x += dpp<XOR1>(x); return x;
}
__device__ __forceinline__ float row16_max(float x) {
  x = fmaxf(x, dpp<ROR8>(x)); x = fmaxf(x, dpp<XOR7>(x)); x = fmaxf(x, dpp<XOR2>(x)); x = fmaxf(x, dpp<XOR1>(x)); return x;
}
__device__ __forceinline__ float tr32(float a, float b) {
  auto s = __builtin_amdgcn_permlane32_swap(asu(a), asu(b), false, false);
  return asf(s[0]) + asf(s[1]);
}
__device__ __forceinline__ float tr16(float a, float b) {
  auto s = __builtin_amdgcn_permlane16_swap(asu(a), asu(b), false, false);
  return asf(s[0]) + asf(s[1]);
}
__device__ __forceinline__ float xrow_sum(float x) { x = tr16(x, x); return tr32(x, x); }
__device__ __forceinline__ float xrow_max(float x) {
  auto s = __builtin_amdgcn_permlane16_swap(asu(x), asu(x), false, false);
  x = fmaxf(asf(s[0]), asf(s[1]));
  auto t = __builtin_amdgcn_permlane32_swap(asu(x), asu(x), false, false);
  return fmaxf(asf(t[0]), asf(t[1]));
}
__device__ __forceinline__ float wave_sum(float x) { return xrow_sum(row16_sum(x)); }
__device__ __forceinline__ float wave_max(float x) { return xrow_max(row16_max(x)); }
__device__ __forceinline__ void tr_reduce8(const float* v, float& o0, float& o1) {
  const float t0 = tr32(v[0], v[4]), t1 = tr32(v[1], v[5]), t2 = tr32(v[2], v[6]), t3 = tr32(v[3], v[7]);
  o0 = tr16(t0, t2); o1 = tr16(t1, t3);
}
__device__ __forceinline__ rsrc_t mk(const void* p, unsigned bytes) { return __builtin_amdgcn_make_buffer_rsrc((void*)p, 0, (int)bytes, 0x00020000); }
template <int AUX> __device__ __forceinline__ u32x4 ldx4(rsrc_t r, unsigned v, unsigned s) { return __builtin_amdgcn_raw_buffer_load_b128(r, v, s, AUX); }
template <int AUX> __device__ __forceinline__ unsigned ldx1(rsrc_t r, unsigned v, unsigned s) { return __builtin_amdgcn_raw_buffer_load_b32(r, v, s, AUX); }
template <int AUX> __device__ __forceinline__ u32x2 ldx2(rsrc_t r, unsigned v, unsigned s) { return __builtin_amdgcn_raw_buffer_load_b64(r, v, s, AUX); }
__device__ __forceinline__ unsigned plo(unsigned a, unsigned b) { return __builtin_amdgcn_perm(b, a, 0x05040100u); }
__device__ __forceinline__ unsigned phi(unsigned a, unsigned b) { return __builtin_amdgcn_perm(b, a, 0x07060302u); }
__device__ __forceinline__ f32x4 mfma(u32x4 a, u32x4 b, f32x4 c) {
  return __builtin_amdgcn_mfma_f32_16x16x32_bf16(__builtin_bit_cast(bf16x8, a), __builtin_bit_cast(bf16x8, b), c, 0, 0, 0);
}
__device__ __forceinline__ void to_frags(const u32x2* L, u32x4* F) {
#pragma unroll
  for (int t = 0; t < 4; ++t) {
    F[0][t] = plo(L[2 * t][0], L[2 * t + 1][0]); F[1][t] = phi(L[2 * t][0], L[2 * t + 1][0]);
    F[2][t] = plo(L[2 * t][1], L[2 * t + 1][1]); F[3][t] = phi(L[2 * t][1], L[2 * t + 1][1]);
  }
}
#define SB() do { asm volatile("" ::: "memory"); __builtin_amdgcn_sched_barrier(0); } while (0)
template <int N> __device__ __forceinline__ void throttle() {
  __builtin_amdgcn_sched_barrier(0);
  if constexpr (N >= 0) asm volatile("s_waitcnt vmcnt(%0)" :: "n"(N) : "memory");
  else asm volatile("" ::: "memory");
}

template <int N, int LIM> __device__ __forceinline__ void throttle_below(int g) {
  __builtin_amdgcn_sched_barrier(0);
  asm volatile("s_cmp_ge_u32 %0, %2\n\ts_cbranch_scc1 1f\n\ts_waitcnt vmcnt(%1)\n1:" :: "s"(g), "n"(N), "n"(LIM) : "memory", "scc");
}

template <int AUX> __device__ __forceinline__ void stx4(u32x4 d, rsrc_t r, unsigned v, unsigned s) {
  __builtin_amdgcn_raw_buffer_store_b128(d, r, v, s, AUX);
  __builtin_amdgcn_sched_barrier(0);
  asm volatile("s_nop 3" :: "v"(d) : "memory");
  __builtin_amdgcn_sched_barrier(0);
}

constexpr unsigned TSTEP = 0x01010101u;
__device__ __forceinline__ unsigned enc(float v, unsigned tb) { return __builtin_amdgcn_perm(asu(v) + 0x80u, tb, 0x07060500u); }
__device__ __forceinline__ float dec(unsigned v) { return asf(v & ~0xffu); }
__device__ __forceinline__ bool tagged(u32x2 t, unsigned tag, int odd) {
  return __builtin_amdgcn_perm(t[1], t[0], 0x0c0c0400u) == ((tag >> (16 * odd)) & 0xffffu);
}

template <int NL, int NH> __device__ __forceinline__ void throttle2(int deep) {
  __builtin_amdgcn_sched_barrier(0);
  asm volatile("s_waitcnt vmcnt(%2)\n\ts_cmp_gt_i32 %0, 0\n\ts_cbranch_scc1 1f\n\ts_waitcnt vmcnt(%1)\n1:" :: "s"(deep), "n"(NL), "n"(NH) : "memory", "scc");
}

struct Args {
  const u16 *x, *k, *v, *wq, *wo, *wg, *wu, *wd;
  unsigned char* ws;
  u16* out;
};
#define STAMP(i) SB()
#define PIN4(A) asm volatile("" : "+v"((A)[0]), "+v"((A)[1]), "+v"((A)[2]), "+v"((A)[3]))

constexpr float SCALE = 0.08838834764831845f, LOG2E = 1.4426950408889634f;
constexpr int wextra(int s) { return (s & 1) == 0 ? 1 : 0; }
constexpr int thr(int j) { int n = wextra(j + 16 - DK); for (int s = j - DK + 1; s < j; ++s) n += 1 + wextra(s + 16); return n; }
constexpr int thd(int j, int dk) { int n = wextra(j + 16 - dk); for (int s = j - dk + 1; s < j; ++s) n += 1 + wextra(s + 16); return n; }
constexpr int brev4(int j) { return ((j & 1) << 3) | ((j & 2) << 1) | ((j & 4) >> 1) | ((j & 8) >> 3); }
__device__ __forceinline__ float trA(float a, float b) {
  float t;
  asm volatile("s_nop 2\n\tv_add_f32_dpp %0, %1, %1 row_ror:8 row_mask:0xf bank_mask:0x3\n\tv_add_f32_dpp %0, %2, %2 row_ror:8 row_mask:0xf bank_mask:0xc"
      : "=&v"(t) : "v"(a), "v"(b));
  return t;
}
__device__ __forceinline__ float trB(float a, float b) {
  float t;
  asm volatile("s_nop 1\n\tv_add_f32_dpp %0, %1, %1 row_half_mirror row_mask:0xf bank_mask:0x5\n\tv_add_f32_dpp %0, %2, %2 row_half_mirror row_mask:0xf bank_mask:0xa\n\ts_nop 1"
      : "=&v"(t) : "v"(a), "v"(b));
  return t;
}
template <int CTRL> __device__ __forceinline__ float trC(float a, float b, bool odd) {
  const float x = a + dpp<CTRL>(a), y = b + dpp<CTRL>(b);
  return odd ? y : x;
}
constexpr int wrow(int t) { return 8 * (t & 3) + (t >> 2); }
constexpr int XS = 648, PS = 64;
__device__ __forceinline__ void to_pair(const u32x2* L, u32x4* F, int t) {
  F[0][t] = plo(L[2 * t][0], L[2 * t + 1][0]); F[1][t] = phi(L[2 * t][0], L[2 * t + 1][0]);
  F[2][t] = plo(L[2 * t][1], L[2 * t + 1][1]); F[3][t] = phi(L[2 * t][1], L[2 * t + 1][1]);
}

__global__ void __launch_bounds__(512, 2) fused_block(Args a) {
  __shared__ __attribute__((aligned(16))) unsigned xd[4 * XS];
  __shared__ __attribute__((aligned(16))) u16 xraw[4 * D_];
  __shared__ __attribute__((aligned(16))) float part[8 * 8 * PS];
  __shared__ __attribute__((aligned(16))) float qg[4 * 128];
  __shared__ __attribute__((aligned(16))) unsigned qw[8 * 64];
  __shared__ __attribute__((aligned(16))) u16 hw[32];
  __shared__ __attribute__((aligned(16))) unsigned alds[16 * 68];
  __shared__ __attribute__((aligned(16))) float red[8 * 16];
  __shared__ unsigned tagl, hflag;
  const int tid = threadIdx.x, lane = tid & 63, w = __builtin_amdgcn_readfirstlane(tid >> 6), r = lane >> 4, c = lane & 15, wl = w & 3;
  unsigned long long pc;
  unsigned tcc;
  asm volatile("s_getpc_b64 %1\n\tglobal_load_dword %0, %2, %1" : "=&v"(tcc), "=&s"(pc) : "v"(min((511 - tid) * 128, PFC)));
  STAMP(0);
  unsigned due = (unsigned)__builtin_amdgcn_s_memrealtime() + TA + 2 * TAU, now;
  int deep = 0;
  asm volatile("" :: "s"(a.x), "s"(a.wd), "s"(a.ws), "s"(a.out));
  const int bid = blockIdx.x, xcd = bid & 7, slot = bid >> 3;
  const int h = xcd * 4 + (slot >> 3), j8 = slot & 7, b = j8 >> 1, half = j8 & 1, u = h * 2 + half;
  const int mat = xcd & 1, rc = xcd >> 1, cg = slot;

  const size_t kvo = ((size_t)(b * H_ + h) * SK + half * 2048) * DH;
  const rsrc_t rK = mk(a.k + kvo, 524288), rV = mk(a.v + kvo, 524288);
  const rsrc_t rWS = mk(a.ws, WS_USED);
  const rsrc_t rZ = mk(a.v, 0);
  const unsigned yb = Y_OFF + b * Y_BLK;
  const unsigned vl16 = lane * 16;
  const int rot = bid;
  auto soff = [&](int i) -> unsigned { return (unsigned)(((i + rot) & 63) * 8192 + w * 1024); };

  u32x4 ring[16];
  const int prow = (w >= 4 ? 160 * b : 160 * rc) + 40 * wl;
  const int lrow = 2 * r + (c >> 3);
  auto project = [&](const u32x4* pw) {
    u32x4 mask;
#pragma unroll
    for (int d = 0; d < 4; ++d) mask[d] = ((c & 7) >> 1) == d ? ((c & 1) ? 0xffff0000u : 0x0000ffffu) : 0u;
    f32x4 D[4];
#pragma unroll
    for (int n = 0; n < 4; ++n) D[n] = f32x4{0.f, 0.f, 0.f, 0.f};
    const unsigned* xp = xd + prow + lrow;
#pragma unroll
    for (int i = 0; i < 5; ++i)
#pragma unroll
      for (int n = 0; n < 4; ++n) D[n] = mfma(pw[i], mask & xp[n * XS + 8 * i], D[n]);
    if ((c >> 3) == (r >> 1)) {
#pragma unroll
      for (int n = 0; n < 4; ++n) *reinterpret_cast<f32x4*>(part + ((w * 2 + (r >> 1)) * 4 + n) * PS + ((r & 1) * 8 + (c & 7)) * 4) = D[n];
    }
  };
  auto publish = [&](unsigned dst, unsigned tag) {
    float q0 = 0.f, q1 = 0.f;
    const float* pp = part + ((w & 4) * 8 + wl) * PS + ((lane >> 5) * 8 + (lane & 7)) * 4 + ((lane >> 3) & 3);
#pragma unroll
    for (int ww = 0; ww < 4; ++ww) { q0 += pp[ww * 8 * PS]; q1 += pp[(ww * 8 + 4) * PS]; }
    __builtin_amdgcn_raw_buffer_store_b32(enc(q0 + q1, tag >> (8 * (lane & 3))), RQG, lane * 4, dst, AUXQS);
  };
#define KPRE(I) { throttle<DK - 1>(); ring[I] = ldx4<AUXK>(rK, vl16, soff(I)); SB(); }
  if (w < 4) {
    const rsrc_t rX = mk(a.x, 4 * D_ * 2);
    const u32x4 xa = ldx4<0>(rX, vl16, w * (D_ * 2));
    const unsigned xb = ldx1<0>(rX, lane * 4, w * (D_ * 2) + 1024);
    SB();
    u32x4 pw[5];
    const rsrc_t rP = mk(mat ? a.wu : a.wg, slot < 27 ? D_ * F_ * 2 : 0);
#pragma unroll
    for (int i = 0; i < 5; ++i) pw[i] = ldx4<AUXP>(rP, lrow * (F_ * 2) + (c & 7) * 16, (prow + 8 * i) * (F_ * 2) + cg * 128);
    SB();
#pragma unroll
    for (int i = 0; i < PX; ++i) ring[i] = ldx4<AUXK>(rK, vl16, soff(i));
    SB();
    float v[10];
#pragma unroll
    for (int i = 0; i < 4; ++i) { v[2 * i] = bflo(xa[i]); v[2 * i + 1] = bfhi(xa[i]); }
    v[8] = bflo(xb); v[9] = bfhi(xb);
    const float mean = wave_sum((((v[0] + v[1]) + (v[2] + v[3])) + ((v[4] + v[5]) + (v[6] + v[7]))) + (v[8] + v[9])) * (1.f / D_);
    float q0 = 0.f, q1 = 0.f;
#pragma unroll
    for (int i = 0; i < 5; ++i) { const float d0 = v[2 * i] - mean, d1 = v[2 * i + 1] - mean; q0 = fmaf(d0, d0, q0); q1 = fmaf(d1, d1, q1); }
    const float var = wave_sum(q0 + q1) * (1.f / D_);
    const float mb = rbf(mean), inv = __builtin_amdgcn_rcpf(rbf(__builtin_amdgcn_sqrtf(rbf(rbf(var) + 1e-5f))));
    unsigned y[10];
#pragma unroll
    for (int i = 0; i < 10; ++i) { const float t = rbf(v[i] - mb) * inv; y[i] = cvtpk(t, t); }
    *reinterpret_cast<u32x4*>(xd + w * XS + lane * 8) = u32x4{y[0], y[1], y[2], y[3]};
    *reinterpret_cast<u32x4*>(xd + w * XS + lane * 8 + 4) = u32x4{y[4], y[5], y[6], y[7]};
    *reinterpret_cast<u32x2*>(xd + w * XS + 512 + lane * 2) = u32x2{y[8], y[9]};
    *reinterpret_cast<u32x4*>(xraw + w * D_ + lane * 8) = xa;
    *reinterpret_cast<unsigned*>(xraw + w * D_ + 512 + lane * 2) = xb;
    __syncthreads();
    STAMP(1);
#pragma unroll
    for (int i = PX; i < PX + PB; ++i) ring[i] = ldx4<AUXK>(rK, vl16, soff(i));
    SB();
    __syncthreads();
    project(pw);
    asm volatile("" :: "v"(tcc));
    STAMP(2);
#pragma unroll
    for (int i = PX + PB; i < NP; ++i) KPRE(i)
    const unsigned tg = __builtin_amdgcn_readfirstlane(tagl), qsrc = Q_OFF + ((b * H_ + h) * 4 + w) * 512;
    u32x2 t = ldx2<AUXQL>(RQG, lane * 8, qsrc);
    SB();
#pragma unroll
    for (int i = NP; i < NP + 3; ++i) { throttle<DK>(); ring[i] = ldx4<AUXK>(rK, vl16, soff(i)); SB(); }
    if (!__all(tagged(t, QTG, lane & 1))) {
#pragma unroll 1
      for (int it = 0; it < (1 << 18); ++it) {
        SB();
        t = ldx2<AUXQL>(RQG, lane * 8, qsrc);
        if (__all(tagged(t, QTG, lane & 1))) break;
        __builtin_amdgcn_s_sleep(1);
      }
    }
    *reinterpret_cast<float2*>(qg + w * 128 + 2 * lane) = make_float2(dec(t[0]), dec(t[1]));
    STAMP(3);
  } else {
    const unsigned ep = ldx1<AUXL>(rWS, 0, EP_OFF);
    SB();
    u32x4 pw[5];
    const rsrc_t rP = mk(a.wq, D_ * 8192);
#pragma unroll
    for (int i = 0; i < 5; ++i) pw[i] = ldx4<AUXP>(rP, lrow * 8192 + (c & 7) * 16, (prow + 8 * i) * 8192 + u * 128);
    SB();
#pragma unroll
    for (int i = 0; i < PA; ++i) ring[i] = ldx4<AUXK>(rK, vl16, soff(i));
    SB();
    tagl = ep + TSTEP;
    hflag = 0;
    __syncthreads();
    STAMP(1);
    project(pw);
    asm volatile("" :: "v"(tcc));
    __syncthreads();
    publish(Q_OFF + ((wl * H_ + h) * 8 + j8) * 256, ep + TSTEP);
    STAMP(2);
#pragma unroll
    for (int i = PA; i < NP + 3; ++i) KPRE(i)
    STAMP(3);
  }
  const unsigned tag = __builtin_amdgcn_readfirstlane(tagl);
  __syncthreads();
  u32x4 qv;
  {
    float q0 = 0.f, q1 = 0.f;
#pragma unroll
    for (int ww = 0; ww < 4; ++ww) { const float2 t = *reinterpret_cast<const float2*>(qg + ww * 128 + 2 * lane); q0 += t.x; q1 += t.y; }
    qw[w * 64 + lane] = cvtpk(q0, q1);
    qv = *reinterpret_cast<const u32x4*>(qw + w * 64 + 4 * c);
  }
  if (w < 4 && slot < 27) publish(G_OFF + (((wl * 2 + mat) * 4 + rc) * GROW + cg * 32) * 8, tag);
#pragma unroll
  for (int i = NP + 3; i < 16; ++i) KPRE(i)
  STAMP(4);

  float s0, s1, s2, s3;
  u32x4 bo[5][4];
  u32x2 L[8];
  u32x2 gu[2];
  float yf[8];
  const unsigned vo = r * (2 * D_ * 2) + (64 * w + 4 * c) * 2;
  const rsrc_t rO = mk(a.wo + (size_t)h * DH * D_, DH * D_ * 2);
#define RWX rW
#define PACE(J) if (((J) & 7) == 0) now = (unsigned)__builtin_amdgcn_s_memrealtime(); if (((J) & 7) == 1) { deep = (int)(now - due); due += TAU; }
#define WLOAD(J) if (((J) & 1) == 0) L[(J) >> 1] = ldx2<AUXW>(RWX, vw, sW + wrow((J) >> 1) * (D_ * 2));
#define ROT(N) { _Pragma("unroll") for (int f = 0; f < 4; ++f) bo[N][f] = bo[(N) + 1][f]; PIN4(bo[N]); }
#define WCONV(J) { if ((J) == 4) { ROT(0) ROT(1) } if ((J) == 5) { ROT(2) ROT(3) } \
      if (((J) & 3) == 3 && (J) > 3) { to_pair(L, bo[4], ((J) >> 2) - 1); PIN4(bo[4]); } }
  {
    const int f0 = (27 * u) & ~3, gc = min(c, (F_ - f0) / 2 - 1), fn = 27 * u + 27 - f0;
    const unsigned vx = r * (2 * D_ * 2) + (512 + 64 * (w >> 2) + 4 * c) * 2;
    const unsigned vg = (((r >> 1) * 4 + (r & 1)) * GROW + gc) * 8;
    const unsigned sg0 = G_OFF + (b * 8 * GROW + (f0 >> 1)) * 8;
    const u16* pd = a.wd + (size_t)f0 * D_;
#pragma unroll 1
    for (int g = 0; g < 4; ++g) {
      s0 = s1; s1 = s2; s2 = s3;
      const rsrc_t rN = g == 3 ? rV : rK;
      const rsrc_t rW = g < 2 ? mk(pd, (g == 0 || wl == 0) ? fn * D_ * 2 : 0) : rO;
      const unsigned vw = (g == 1 || g == 2) ? vx : vo;
      const unsigned sW = g == 2 ? wl * (32 * D_ * 2) : 0;
      float ta, tb, tc, td;
#define KSTEP(J) { const u32x4 kv = ring[J]; float d = dot2(kv[0], qv[0], 0.f); d = dot2(kv[1], qv[1], d); d = dot2(kv[2], qv[2], d); d = dot2(kv[3], qv[3], d); \
      asm volatile("" : "+v"(d)); \
      PACE(J) throttle2<thd(J, DL), thd(J, DH2)>(deep); \
      ring[J] = ldx4<AUXK>(rN, vl16, soff(g * 16 + (J) + 16)); \
      WLOAD(J) \
      if ((J) == 7 && g == 1 && w == 0) { gu[0] = ldx2<AUXQL>(RQG, vg, sg0); gu[1] = ldx2<AUXQL>(RQG, vg, sg0 + 2 * GROW * 8); } \
      SB(); \
      if (((J) & 1) == 0) ta = d; else { const float t1 = trA(ta, d); \
        if (((J) & 2) == 0) tb = t1; else { const float t2 = trB(tb, t1); \
          if (((J) & 4) == 0) tc = t2; else { const float t3 = trC<XOR2>(tc, t2, c & 2); \
            if (((J) & 8) == 0) td = t3; else s3 = trC<XOR1>(td, t3, c & 1); } } } \
      if ((J) == 3) { to_pair(L, bo[4], 3); PIN4(bo[4]); } \
      WCONV(J) }
      KSTEP(0) KSTEP(1)
      if (g == 2 && w == 0) {
        auto guok = [&]() { return __all(tagged(gu[0], QTAG, gc & 1) && tagged(gu[1], QTAG, gc & 1)); };
        if (!guok()) {
#pragma unroll 1
          for (int it = 0; it < (1 << 18); ++it) {
            __builtin_amdgcn_s_sleep(2);
            SB();
            gu[0] = ldx2<AUXQL>(RQG, vg, sg0); gu[1] = ldx2<AUXQL>(RQG, vg, sg0 + 2 * GROW * 8);
            if (guok()) break;
          }
        }
        const float gs = rbf(tr16(dec(gu[0][0]) + dec(gu[1][0]), dec(gu[0][1]) + dec(gu[1][1])));
        auto sw = __builtin_amdgcn_permlane32_swap(asu(gs), asu(gs), false, false);
        const float gg = asf(sw[0]), uu = asf(sw[1]);
        const float sg = rbf(gg * __builtin_amdgcn_rcpf(1.f + __builtin_amdgcn_exp2f(-gg * LOG2E)));
        const int kk = 2 * c + (r & 1);
        const float hv = (kk >= fn - 27 && kk < fn) ? sg * uu : 0.f;
        if (lane < 32) hw[4 * (kk & 7) + (kk >> 3)] = (u16)cvtpk(hv, hv);
        __hip_atomic_store(&hflag, 1u, __ATOMIC_RELEASE, __HIP_MEMORY_SCOPE_WORKGROUP);
      }
      if (g == 3) {
#pragma unroll 1
        for (int it = 0; it < (1 << 24); ++it) {
          if (__hip_atomic_load(&hflag, __ATOMIC_ACQUIRE, __HIP_MEMORY_SCOPE_WORKGROUP)) break;
          __builtin_amdgcn_s_sleep(2);
        }
        u32x4 ah = *reinterpret_cast<const u32x4*>(hw + 8 * r);
        if (c != 0) ah = u32x4{0u, 0u, 0u, 0u};
        const f32x4 z = {0.f, 0.f, 0.f, 0.f};
#pragma unroll
        for (int f = 0; f < 4; ++f) { yf[f] = mfma(ah, bo[2][f], z)[0]; yf[4 + f] = mfma(ah, bo[3][f], z)[0]; }
        asm volatile("" : "+v"(yf[0]), "+v"(yf[1]), "+v"(yf[2]), "+v"(yf[3]), "+v"(yf[4]), "+v"(yf[5]), "+v"(yf[6]), "+v"(yf[7]));
      }
      KSTEP(2) KSTEP(3) KSTEP(4) KSTEP(5) KSTEP(6) KSTEP(7)
      KSTEP(8) KSTEP(9) KSTEP(10) KSTEP(11) KSTEP(12) KSTEP(13) KSTEP(14) KSTEP(15)
    }
  }
  STAMP(5);
  s0 = rbf(rbf(s0) * SCALE); s1 = rbf(rbf(s1) * SCALE); s2 = rbf(rbf(s2) * SCALE); s3 = rbf(rbf(s3) * SCALE);
  const float mw = wave_max(fmaxf(fmaxf(s0, s1), fmaxf(s2, s3)));
  unsigned p0, p1, p2, p3;
  const unsigned mlo = yb + ML_OFF + h * 256;
  {
    const float e0 = __builtin_amdgcn_exp2f((s0 - mw) * LOG2E), e1 = __builtin_amdgcn_exp2f((s1 - mw) * LOG2E);
    const float e2 = __builtin_amdgcn_exp2f((s2 - mw) * LOG2E), e3 = __builtin_amdgcn_exp2f((s3 - mw) * LOG2E);
    p0 = cvtpk(e0, e0 - rbf(e0)); p1 = cvtpk(e1, e1 - rbf(e1)); p2 = cvtpk(e2, e2 - rbf(e2)); p3 = cvtpk(e3, e3 - rbf(e3));
    const float lw = wave_sum((e0 + e1) + (e2 + e3));
    stx4<AUXS>(u32x4{asu(mw), tag, asu(lw), tag}, rWS, lane == 0 ? 0u : 0x80000000u, mlo + (half * 8 + w) * 16);
  }
  STAMP(6);
  f32x4 acc0 = {0.f, 0.f, 0.f, 0.f}, acc1 = acc0;
  u32x4 sel;
#pragma unroll
  for (int d = 0; d < 4; ++d) {
    const unsigned hs = (c & 8) ? 0x0302u : 0x0100u;
    sel[d] = ((c & 7) >> 1) == d ? ((c & 1) ? (hs << 16) | 0x0c0cu : 0x0c0c0000u | hs) : 0x0c0c0c0cu;
  }
  float scale = 0.f;
  bool mlok = false;
  auto mlcalc = [&](u32x4 ml) {
    asm volatile("" : "+v"(ml));
    mlok = __all(ml[1] == tag && ml[3] == tag);
    const float mi = asf(ml[0]), M = row16_max(mi);
    const float L = row16_sum(__builtin_amdgcn_exp2f((mi - M) * LOG2E) * asf(ml[2]));
    scale = __builtin_amdgcn_exp2f((mw - M) * LOG2E) / L;
    asm volatile("" : "+v"(scale));
  };
  u32x4 ml = {0u, 0u, 0u, 0u};
#define VCOMP(J) { const unsigned pb = __builtin_amdgcn_mov_dpp(p0, 0x150 + brev4(J), 0xf, 0xf, true); u32x4 pm; \
      _Pragma("unroll") for (int d = 0; d < 4; ++d) pm[d] = __builtin_amdgcn_perm(pb, pb, sel[d]); \
      if ((J) & 1) { acc1 = mfma(ring[J], pm, acc1); asm volatile("" : "+v"(acc1)); } else { acc0 = mfma(ring[J], pm, acc0); asm volatile("" : "+v"(acc0)); } }
#pragma unroll 1
  for (int g = 0; g < 3; ++g) {
    const rsrc_t rW = rO;
    const unsigned vw = vo, sW = (g + 1) * (32 * D_ * 2);
#define VSTEP(J) { VCOMP(J) \
      PACE(J) if ((J) == VDEEP && g == 2) due = now - 0x10000000u, deep = 1; \
      throttle2<thd(J, DL), thd(J, DH2)>(deep); \
      ring[J] = ldx4<AUXV>(rV, vl16, soff(g * 16 + (J) + 16)); \
      WLOAD(J) \
      if ((J) == 1 && g == 2) ml = ldx4<AUXL>(rWS, c * 16, mlo); \
      if ((J) == FS && g == 2 && w == 0) __builtin_amdgcn_raw_buffer_store_b64(u32x2{tag, tag}, RY, lane == 0 ? 0u : 0x80000000u, yb + FL_OFF + u * 8, AUXS); \
      SB(); \
      if ((J) == 9 && g == 2) mlcalc(ml); \
      if ((J) == 3) { to_pair(L, bo[4], 3); PIN4(bo[4]); } \
      WCONV(J) }
    VSTEP(0) VSTEP(1) VSTEP(2) VSTEP(3) VSTEP(4) VSTEP(5) VSTEP(6) VSTEP(7)
    VSTEP(8) VSTEP(9) VSTEP(10) VSTEP(11) VSTEP(12) VSTEP(13) VSTEP(14) VSTEP(15)
    p0 = p1; p1 = p2; p2 = p3;
  }
#pragma unroll 1
  for (int it = 0; !mlok && it < (1 << 18); ++it) {
    SB();
    __builtin_amdgcn_s_sleep(2);
    mlcalc(ldx4<AUXL>(rWS, c * 16, mlo));
  }
  VCOMP(0) VCOMP(1) VCOMP(2) VCOMP(3) VCOMP(4) VCOMP(5) VCOMP(6) VCOMP(7)
  VCOMP(8) VCOMP(9) VCOMP(10) VCOMP(11) SB(); VCOMP(12) SB(); VCOMP(13) SB(); VCOMP(14) SB();
  to_pair(L, bo[4], 3); PIN4(bo[4]);
  VCOMP(15)
  STAMP(7);
  {
    float o[4];
#pragma unroll
    for (int i = 0; i < 4; ++i) { const float t = acc0[i] + acc1[i]; o[i] = (t + dpp<ROR8>(t)) * scale; }
    const unsigned h0 = cvtpk(o[0], o[1]), h1 = cvtpk(o[2], o[3]);
    const unsigned l0 = cvtpk(o[0] - bflo(h0), o[1] - bfhi(h0)), l1 = cvtpk(o[2] - bflo(h1), o[3] - bfhi(h1));
    *reinterpret_cast<u32x2*>(alds + (w + (c & 8)) * 68 + 16 * r + 2 * (c & 7)) = (c & 8) ? u32x2{l0, l1} : u32x2{h0, h1};
  }
  __syncthreads();
  STAMP(8);
  {
    u32x4 af[5];
#pragma unroll
    for (int s = 0; s < 4; ++s) af[s] = *reinterpret_cast<const u32x4*>(alds + c * 68 + 16 * s + 4 * r);
    af[4] = *reinterpret_cast<const u32x4*>(alds + c * 68 + 16 * (w & 3) + 4 * r);
    f32x4 C[8];
#pragma unroll
    for (int f = 0; f < 8; ++f) C[f] = f32x4{yf[f], 0.f, 0.f, 0.f};
#pragma unroll
    for (int s = 0; s < 4; ++s)
#pragma unroll
      for (int f = 0; f < 4; ++f) C[f] = mfma(af[s], bo[s + 1][f], C[f]);
#pragma unroll
    for (int f = 0; f < 4; ++f) C[4 + f] = mfma(af[4], bo[0][f], C[4 + f]);
    float z[8];
#pragma unroll
    for (int f = 0; f < 8; ++f) z[f] = (C[f][0] + C[f][1]) + (C[f][2] + C[f][3]);
    float y0, y1; tr_reduce8(z, y0, y1);
    const unsigned gi = r < 2 ? 64 * w + 4 * c + 2 * r : 512 + ((w >> 2) * 16 + c) * 16 + (w & 3) * 4 + 2 * (r - 2);
    __builtin_amdgcn_raw_buffer_store_b64(u32x2{enc(y0, tag >> (16 * (r & 1))), enc(y1, tag >> (16 * (r & 1) + 8))}, RY, gi * 4, yb + u * Y_STRIDE, AUXS);
  }
  STAMP(9);
  {
    const unsigned vy = (tid >> 3) * Y_STRIDE + (tid & 7) * 8;
    float y0 = 0.f, y1 = 0.f;
    bool ok = false;
#pragma unroll 1
    for (int it = 0; it < (1 << 18); ++it) {
      SB();
      const u32x2 t = ldx2<AUXL>(RY, (lane & 7) * 8, yb + FL_OFF + w * 64);
      if (__all(t[0] == YTAG && t[1] == YTAG)) break;
      __builtin_amdgcn_s_sleep(FSLEEP);
    }
#pragma unroll 1
    for (int it = 0; it < (1 << 18); ++it) {
      SB();
      if (!ok) {
        const u32x2 t = ldx2<AUXL>(RY, vy, yb + u * 64);
        ok = tagged(t, YTAG, lane & 1);
        y0 = dec(t[0]); y1 = dec(t[1]);
      }
      const unsigned long long bal = __ballot(ok);
      if (bal == ~0ull) break;
      ok = ((bal >> (lane & 56)) & 0xff) == 0xff;
      __builtin_amdgcn_s_sleep(YSLEEP);
    }
    float y = tr32(y0, y1);
    y = tr16(y, y);
    y += dpp<ROR8>(y);
    if ((lane & 24) == 0) red[w * 16 + 2 * (lane & 7) + (lane >> 5)] = y;
  }
  STAMP(10);
  __syncthreads();
  if (tid < 16) {
    const int ocol = u < 32 ? 16 * u + tid : 512 + 4 * (u - 32) + (tid & 3);
    float y = 0.f;
#pragma unroll
    for (int ww = 0; ww < 8; ++ww) y += red[ww * 16 + tid];
    if (u >= 32) { y += dpp<ROR4>(y); y += dpp<ROR8>(y); }
    if (u < 32 || tid < 4) a.out[b * D_ + ocol] = (u16)cvtpk(bflo(xraw[b * D_ + ocol]) + y, 0.f);
    if (bid == 0 && tid == 0) __builtin_amdgcn_raw_buffer_store_b32(tag, rWS, 0, EP_OFF, AUXS);
  }
}

extern "C" void kernel_launch(void* const* d_in, const int* in_sizes, int n_in, void* d_out, int out_size, void* d_ws, size_t ws_size,
                              hipStream_t stream) {
  Args a;
  a.x = (const u16*)d_in[0]; a.k = (const u16*)d_in[1]; a.v = (const u16*)d_in[2];
  a.wq = (const u16*)d_in[3]; a.wo = (const u16*)d_in[4];
  a.wg = (const u16*)d_in[6]; a.wu = (const u16*)d_in[7]; a.wd = (const u16*)d_in[8];
  a.ws = (unsigned char*)d_ws; a.out = (u16*)d_out;
  fused_block<<<256, 512, 0, stream>>>(a);
}
```
